```python
import torch

def _softmax(x, axis=-1):
    return torch.softmax(x, dim=axis)
def _gelu(x):
    return torch.nn.functional.gelu(x, approximate="tanh")
def _silu(x):
    return torch.nn.functional.silu(x)
def _sigmoid(x):
    return torch.sigmoid(x)
def _layernorm(x, axis=-1, eps=1e-5):
    m = x.mean(dim=axis, keepdim=True)
    v = x.var(dim=axis, keepdim=True, unbiased=False)
    return (x - m) / torch.sqrt(v + eps)
def _scatter_add(tgt, idx, src, axis=0):
    return tgt.clone().index_add_(axis, idx, src)
def _segment_sum(src, seg, n):
    out = torch.zeros((n,) + src.shape[1:], dtype=src.dtype)
    return out.index_add_(0, seg, src)
def _pad(x, pw):
    flat = tuple(v for pair in reversed(pw) for v in pair)
    return torch.nn.functional.pad(x, flat)

kernel_name = 'interleave_layers__grouped_kv_mqa__glu_gated_geglu__prompt'

def setup_inputs():

    _w = (lambda a: (lambda t: t.to(torch.bfloat16) if t.is_floating_point() else t)(torch.as_tensor(a)))
    def _A_setup():
        B, S, D, H = 1, 2048, 640, 32
        Dh = D // H
        return {
            "x": _w((lambda *s: torch.randn(*s, dtype=torch.bfloat16) * 0.05)(B, S, D)),
            "wq": _w((lambda *s: torch.randn(*s, dtype=torch.bfloat16) * 0.05)(D, H * Dh)),
            "wk": _w((lambda *s: torch.randn(*s, dtype=torch.bfloat16) * 0.05)(D, Dh)),
            "wv": _w((lambda *s: torch.randn(*s, dtype=torch.bfloat16) * 0.05)(D, Dh)),
            "wo": _w((lambda *s: torch.randn(*s, dtype=torch.bfloat16) * 0.05)(H * Dh, D)),
        }
    _A_in = _A_setup()

    def _B_setup():
        B, S, Dm, Df = 1, 2048, 640, 1728
        return {
            "x": _w((lambda *s: torch.randn(*s, dtype=torch.bfloat16) * 0.05)(B, S, Dm)),
            "w_gate": _w((lambda *s: torch.randn(*s, dtype=torch.bfloat16) * 0.8)(Dm, Df) * (Dm ** -0.5)),
            "w_up": _w((lambda *s: torch.randn(*s, dtype=torch.bfloat16) * 0.8)(Dm, Df) * (Dm ** -0.5)),
            "w_down": _w((lambda *s: torch.randn(*s, dtype=torch.bfloat16) * 0.8)(Df, Dm) * (Df ** -0.5)),
        }
    _B_in = _B_setup()

    return {
        "A_x": _A_in["x"],
        "A_wq": _A_in["wq"],
        "A_wk": _A_in["wk"],
        "A_wv": _A_in["wv"],
        "A_wo": _A_in["wo"],
        "B_x": _B_in["x"],
        "B_w_gate": _B_in["w_gate"],
        "B_w_up": _B_in["w_up"],
        "B_w_down": _B_in["w_down"],
    }

def reference(A_x, A_wq, A_wk, A_wv, A_wo, B_x, B_w_gate, B_w_up, B_w_down):
    def _A_ref(x, wq, wk, wv, wo):
        B, S, D = x.shape
        H = 32
        Dh = D // H
        q = (lambda x, *d: x.permute(*d))((x @ wq).reshape(B, S, H, Dh), 0, 2, 1, 3)
        k = x @ wk
        v = x @ wv
        scores = torch.einsum("bhqd,bkd->bhqk", q, k) * (Dh ** -0.5)
        p = _softmax(scores, axis=-1)
        o = torch.einsum("bhqk,bkd->bhqd", p, v)
        return (lambda x, *d: x.permute(*d))(o, 0, 2, 1, 3).reshape(B, S, H * Dh) @ wo

    def _B_ref(x, w_gate, w_up, w_down):
        g = torch.einsum("bsd,df->bsf", x, w_gate)
        u = torch.einsum("bsd,df->bsf", x, w_up)
        h = _gelu(g) * u
        return torch.einsum("bsf,fd->bsd", h, w_down)

    x = A_x
    x = x + _A_ref(_layernorm(x, axis=-1), A_wq, A_wk, A_wv, A_wo)
    x = x + _B_ref(_layernorm(x, axis=-1), B_w_gate, B_w_up, B_w_down)
    x = x + _A_ref(_layernorm(x, axis=-1), A_wq, A_wk, A_wv, A_wo)
    x = x + _B_ref(_layernorm(x, axis=-1), B_w_gate, B_w_up, B_w_down)
    return x
```

```cpp
#include <hip/hip_runtime.h>
#include <hip/hip_bf16.h>
#include <stdint.h>

typedef short    bf16x8 __attribute__((ext_vector_type(8)));
typedef short    s16x4  __attribute__((ext_vector_type(4)));
typedef float    f32x4  __attribute__((ext_vector_type(4)));
typedef float    f32x16 __attribute__((ext_vector_type(16)));
typedef unsigned u32x4  __attribute__((ext_vector_type(4)));
typedef unsigned u32x2  __attribute__((ext_vector_type(2)));
typedef unsigned short u16;

namespace {

constexpr int SEQ = 2048, DM = 640, DFF = 1728;
constexpr float QSCALE = 0.22360679774997896f * 1.4426950408889634f;
constexpr float LN_EPS = 1e-5f;

constexpr size_t ALN(size_t x) { return (x + 255) / 256 * 256; }
constexpr size_t WS_XA  = 0;
constexpr size_t WS_XB  = WS_XA + ALN((size_t)SEQ * DM * 2);
constexpr size_t WS_QS  = WS_XB + ALN((size_t)SEQ * DM * 2);
constexpr size_t WS_OB  = WS_QS + ALN((size_t)SEQ * DM * 2);
constexpr size_t WS_HB  = WS_OB + ALN((size_t)SEQ * DM * 2);
constexpr size_t WS_KF  = WS_HB + ALN((size_t)SEQ * DFF * 2);
constexpr size_t WS_VF  = WS_KF + 131072;
constexpr size_t WS_WKV = WS_VF + 131072;
constexpr size_t WS_CSQ = WS_WKV + ALN(640 * 48 * 2);
constexpr size_t WS_CSG = WS_CSQ + ALN(688 * 4);
constexpr size_t WS_CSU = WS_CSG + ALN(DFF * 4);
constexpr size_t WS_ST0 = WS_CSU + ALN(DFF * 4);
constexpr size_t WS_STA = WS_ST0 + ALN(SEQ * 8);
constexpr size_t WS_STB = WS_STA + ALN(8 * SEQ * 8);
constexpr size_t WS_END = WS_STB + ALN(8 * SEQ * 8);

__device__ __forceinline__ float bf2f(unsigned v) { return __uint_as_float(v << 16); }
__device__ __forceinline__ float bfhi(unsigned v) { return __uint_as_float(v & 0xffff0000u); }
__device__ __forceinline__ unsigned cvtpk(float lo, float hi) {
  unsigned r; asm("v_cvt_pk_bf16_f32 %0, %1, %2" : "=v"(r) : "v"(lo), "v"(hi)); return r;
}
__device__ __forceinline__ float rbf(float x) { return __uint_as_float(cvtpk(0.f, x)); }

template <int N> __device__ __forceinline__ void wait_vm() { asm volatile("s_waitcnt vmcnt(%0)" :: "i"(N) : "memory"); }
__device__ __forceinline__ void raw_barrier() {
  asm volatile("" ::: "memory"); __builtin_amdgcn_s_barrier(); asm volatile("" ::: "memory");
}
__device__ __forceinline__ void glds16(const void* g, char* lds) {
#if defined(__HIP_DEVICE_COMPILE__)
  __builtin_amdgcn_global_load_lds((const __attribute__((address_space(1))) void*)g,
                                   (__attribute__((address_space(3))) void*)lds, 16, 0, 0);
#endif
}
__device__ __forceinline__ bf16x8 tr_frag(const char* p0, const char* p1) {
  bf16x8 r = {};
#if defined(__HIP_DEVICE_COMPILE__)
  s16x4 lo = __builtin_amdgcn_ds_read_tr16_b64_v4i16((__attribute__((address_space(3))) s16x4*)p0);
  s16x4 hi = __builtin_amdgcn_ds_read_tr16_b64_v4i16((__attribute__((address_space(3))) s16x4*)p1);
  r = __builtin_shufflevector(lo, hi, 0, 1, 2, 3, 4, 5, 6, 7);
#endif
  return r;
}
__device__ __forceinline__ f32x4 mfma16(bf16x8 a, bf16x8 b, f32x4 c) {
  return __builtin_amdgcn_mfma_f32_16x16x32_bf16(a, b, c, 0, 0, 0);
}
__device__ __forceinline__ f32x16 mfma32(bf16x8 a, bf16x8 b, f32x16 c) {
  return __builtin_amdgcn_mfma_f32_32x32x16_bf16(a, b, c, 0, 0, 0);
}

__global__ __launch_bounds__(256) void k0_prep(const u16* __restrict__ x, const u16* __restrict__ wq,
                                               const u16* __restrict__ wk, const u16* __restrict__ wv,
                                               const u16* __restrict__ wg, const u16* __restrict__ wu,
                                               float2* __restrict__ st0, float* __restrict__ csq,
                                               float* __restrict__ csg, float* __restrict__ csu,
                                               u16* __restrict__ wkv) {
  __shared__ float red[8][256];
  int b = blockIdx.x;
  const int t = threadIdx.x;
  if (b < 512) {
    const int row = b * 4 + (t >> 6), l = t & 63;
    const unsigned* xr = (const unsigned*)(x + (size_t)row * DM);
    float s = 0.f, q = 0.f;
#pragma unroll
    for (int j = 0; j < 5; ++j) {
      const unsigned v = xr[j * 64 + l];
      const float a = bf2f(v), c = bfhi(v);
      s += a + c; q += a * a + c * c;
    }
#pragma unroll
    for (int o = 32; o > 0; o >>= 1) { s += __shfl_xor(s, o); q += __shfl_xor(q, o); }
    if (l == 0) st0[row] = make_float2(s, q);
    return;
  }
  b -= 512;
  if (b < 17) {
    const u16* W; int N; float* out; int cb;
    if (b < 3)       { W = wq; N = DM;  out = csq; cb = b; }
    else if (b < 10) { W = wg; N = DFF; out = csg; cb = b - 3; }
    else             { W = wu; N = DFF; out = csu; cb = b - 10; }
    const int cg = t & 31, rg = t >> 5, col = cb * 256 + cg * 8;
    float a[8];
#pragma unroll
    for (int j = 0; j < 8; ++j) a[j] = 0.f;
    if (col < N) {
      for (int r = rg * 80; r < rg * 80 + 80; ++r) {
        const u32x4 v = *(const u32x4*)(W + (size_t)r * N + col);
#pragma unroll
        for (int j = 0; j < 4; ++j) { a[2 * j] += bf2f(v[j]); a[2 * j + 1] += bfhi(v[j]); }
      }
    }
#pragma unroll
    for (int j = 0; j < 8; ++j) red[rg][cg * 8 + j] = a[j];
    __syncthreads();
    const int c = cb * 256 + t;
    if (c < N) {
      float s = 0.f;
#pragma unroll
      for (int r = 0; r < 8; ++r) s += red[r][t];
      out[c] = s;
    }
    return;
  }
  {
    float s = 0.f;
    const int c = t % 48, rg = t / 48;
    if (t < 240) {
      for (int k = rg * 128; k < rg * 128 + 128; ++k) {
        u16 v = 0;
        if (c < 20) v = wk[k * 20 + c];
        else if (c < 40) v = wv[k * 20 + c - 20];
        wkv[k * 48 + c] = v;
        s += bf2f(v);
      }
      red[rg][c] = s;
    }
    __syncthreads();
    if (t < 48) csq[640 + t] = red[0][t] + red[1][t] + red[2][t] + red[3][t] + red[4][t];
  }
}

struct GArgs {
  const u16* X;
  const u16* W0;
  const u16* W1;
  const u16* res;
  u16* out;
  const float2* st_in;
  float2* st_out;
  const float* cs0;
  const float* cs1;
  char* kf;
  char* vf;
  int K;
  int ns_in;
};

enum { EPI_QKV = 0, EPI_RES = 1 };

template <int LH, int C> __device__ __forceinline__ void wait_steady(int L) {
  if (L == LH) wait_vm<LH * C>();
  else wait_vm<(LH - 1) * C>();
}

__device__ __forceinline__ float2 ln_row(const float2* st, int ns, int row) {
  float s = 0.f, q = 0.f;
  for (int i = 0; i < ns; ++i) { const float2 v = st[(size_t)i * SEQ + row]; s += v.x; q += v.y; }
  const float mean = s * (1.f / DM);
  float var = q * (1.f / DM) - mean * mean;
  var = var < 0.f ? 0.f : var;
  return make_float2(mean, __builtin_amdgcn_rsqf(var + LN_EPS));
}

template <int EPI, int NF>
__device__ __forceinline__ void small_body(const GArgs& a, char* smem) {
  constexpr int BM = 64, NW = 4, NSTAGE = 3;
  constexpr int XI = BM / 8, T = XI + 2 * NF, LH = (T + NW - 1) / NW;
  constexpr int XBYTES = BM * 128, STAGE = XBYTES + NF * 2048;
  const int tid = threadIdx.x, lane = tid & 63;
  const int wid = __builtin_amdgcn_readfirstlane(tid >> 6);
  const int wm = wid & 1, wk = wid >> 1;
  const int g = lane >> 4;
  const int m0 = blockIdx.x * BM, n0 = blockIdx.y * 80;
  const int K = a.K, nkt = K >> 6;
  float2* ms = (float2*)(smem + NSTAGE * STAGE);

  if (EPI == EPI_QKV) {
    if (tid < BM) ms[tid] = ln_row(a.st_in, a.ns_in, m0 + tid);
  }

  const char* src[LH];
  unsigned adv[LH];
  const int L = (T - wid + NW - 1) / NW;
#pragma unroll
  for (int j = 0; j < LH; ++j) {
    const int i = wid + j * NW;
    src[j] = nullptr; adv[j] = 0;
    if (i < XI) {
      const int P = i * 64 + lane, m = P >> 3, kc = (P & 7) ^ (m & 7);
      src[j] = (const char*)(a.X + (size_t)(m0 + m) * K + kc * 8);
      adv[j] = 128;
    } else if (i < T) {
      const int P = (i - XI) * 64 + lane, b = P >> 4, c = P & 15;
      const int o = b / NF, nf = b - o * NF;
      const int r = (c >> 3) ^ ((o ^ nf) & 1);
      const int k = o * 8 + r * 4 + ((c >> 1) & 3), n = nf * 16 + (c & 1) * 8;
      if (EPI == EPI_QKV && nf >= 5) {
        src[j] = (const char*)(a.W1 + (size_t)k * 48 + (n - 80));
        adv[j] = 64 * 48 * 2;
      } else {
        src[j] = (const char*)(a.W0 + (size_t)k * DM + n0 + n);
        adv[j] = 64 * DM * 2;
      }
    }
  }
  auto issue = [&](int buf) {
#pragma unroll
    for (int j = 0; j < LH; ++j)
      if (j < L) { glds16(src[j], smem + buf * STAGE + (wid + j * NW) * 1024); src[j] += adv[j]; }
  };

  int xoff[2];
#pragma unroll
  for (int mf = 0; mf < 2; ++mf) {
    const int m = wm * 32 + mf * 16 + (lane & 15);
    xoff[mf] = m * 128 + (((4 * wk + g) ^ (m & 7)) << 4);
  }
  const int wbase = XBYTES + (4 * wk + g) * NF * 256 + ((lane >> 2) & 3) * 32 + (lane & 3) * 8;
  const int wa0 = wbase + (g & 1) * 128, wa1 = wbase + 128 - (g & 1) * 128;

  f32x4 acc[NF][2];
#pragma unroll
  for (int nf = 0; nf < NF; ++nf) { acc[nf][0] = f32x4{0.f, 0.f, 0.f, 0.f}; acc[nf][1] = acc[nf][0]; }

  issue(0); issue(1);
  int buf = 0;
  for (int t = 0; t < nkt; ++t) {
    if (t + 1 < nkt) wait_steady<LH, 1>(L); else wait_vm<0>();
    raw_barrier();
    int nb = buf + 2; nb = nb >= NSTAGE ? nb - NSTAGE : nb;
    if (t + 2 < nkt) issue(nb);
    const char* sb = smem + buf * STAGE;
    bf16x8 xf[2];
#pragma unroll
    for (int mf = 0; mf < 2; ++mf) xf[mf] = *(const bf16x8*)(sb + xoff[mf]);
#pragma unroll
    for (int nf = 0; nf < NF; ++nf) {
      const bf16x8 wf = (nf & 1) ? tr_frag(sb + wa1 + nf * 256, sb + wa0 + nf * 256)
                                 : tr_frag(sb + wa0 + nf * 256, sb + wa1 + nf * 256);
#pragma unroll
      for (int mf = 0; mf < 2; ++mf) acc[nf][mf] = mfma16(wf, xf[mf], acc[nf][mf]);
    }
    buf = buf + 1 >= NSTAGE ? 0 : buf + 1;
  }

  __syncthreads();
  float* red = (float*)smem;
  if (wk == 1) {
#pragma unroll
    for (int nf = 0; nf < NF; ++nf)
#pragma unroll
      for (int mf = 0; mf < 2; ++mf)
        *(f32x4*)(red + (((wm * NF + nf) * 2 + mf) * 64 + lane) * 4) = acc[nf][mf];
  }
  __syncthreads();
  if (wk == 1) return;
#pragma unroll
  for (int nf = 0; nf < NF; ++nf)
#pragma unroll
    for (int mf = 0; mf < 2; ++mf)
      acc[nf][mf] += *(const f32x4*)(red + (((wm * NF + nf) * 2 + mf) * 64 + lane) * 4);

  if (EPI == EPI_RES) {
    float ssum[2] = {0.f, 0.f}, ssq[2] = {0.f, 0.f};
#pragma unroll
    for (int nf = 0; nf < NF; ++nf) {
      const int n = n0 + nf * 16 + 4 * g;
#pragma unroll
      for (int mf = 0; mf < 2; ++mf) {
        const int m = m0 + wm * 32 + mf * 16 + (lane & 15);
        const u32x2 xo = *(const u32x2*)(a.res + (size_t)m * DM + n);
        const float v0 = bf2f(xo[0]) + rbf(acc[nf][mf][0]);
        const float v1 = bfhi(xo[0]) + rbf(acc[nf][mf][1]);
        const float v2 = bf2f(xo[1]) + rbf(acc[nf][mf][2]);
        const float v3 = bfhi(xo[1]) + rbf(acc[nf][mf][3]);
        const u32x2 pk = {cvtpk(v0, v1), cvtpk(v2, v3)};
        *(u32x2*)(a.out + (size_t)m * DM + n) = pk;
        const float f0 = bf2f(pk[0]), f1 = bfhi(pk[0]), f2 = bf2f(pk[1]), f3 = bfhi(pk[1]);
        ssum[mf] += (f0 + f1) + (f2 + f3);
        ssq[mf] += (f0 * f0 + f1 * f1) + (f2 * f2 + f3 * f3);
      }
    }
#pragma unroll
    for (int mf = 0; mf < 2; ++mf) {
      float s = ssum[mf], q = ssq[mf];
      s += __shfl_xor(s, 16); q += __shfl_xor(q, 16);
      s += __shfl_xor(s, 32); q += __shfl_xor(q, 32);
      if (lane < 16) a.st_out[(size_t)blockIdx.y * SEQ + m0 + wm * 32 + mf * 16 + lane] = make_float2(s, q);
    }
  } else {
    float2 mr[2];
#pragma unroll
    for (int mf = 0; mf < 2; ++mf) mr[mf] = ms[wm * 32 + mf * 16 + (lane & 15)];
#pragma unroll
    for (int nf = 0; nf < NF; ++nf) {
      if (nf < 5) {
        const int n = n0 + nf * 16 + 4 * g;
        const f32x4 cs = *(const f32x4*)(a.cs0 + n);
#pragma unroll
        for (int mf = 0; mf < 2; ++mf) {
          const int m = m0 + wm * 32 + mf * 16 + (lane & 15);
          const float sc = mr[mf].y * QSCALE;
          float v[4];
#pragma unroll
          for (int j = 0; j < 4; ++j) v[j] = sc * (acc[nf][mf][j] - mr[mf].x * cs[j]);
          const u32x2 pk = {cvtpk(v[0], v[1]), cvtpk(v[2], v[3])};
          *(u32x2*)(a.out + (size_t)m * DM + n) = pk;
        }
      } else {
        const int kvf = nf - 5;
        const f32x4 cs = *(const f32x4*)(a.cs0 + 640 + kvf * 16 + 4 * g);
#pragma unroll
        for (int mf = 0; mf < 2; ++mf) {
          const int m = m0 + wm * 32 + mf * 16 + (lane & 15);
          float v[4];
#pragma unroll
          for (int j = 0; j < 4; ++j) v[j] = mr[mf].y * (acc[nf][mf][j] - mr[mf].x * cs[j]);
          const u32x2 pk = {cvtpk(v[0], v[1]), cvtpk(v[2], v[3])};
          const int T5 = m >> 5, r = m & 31;
          int dv = -1;
          if (kvf == 0) {
            *(u32x2*)(a.kf + T5 * 2048 + (r + 32 * (g >> 1)) * 16 + (g & 1) * 8) = pk;
          } else if (kvf == 1) {
            if (g == 0) {
              const u32x4 w = {pk[0], pk[1], 0x3f80u, 0u};
              *(u32x4*)(a.kf + T5 * 2048 + 1024 + r * 16) = w;
              *(u32x4*)(a.kf + T5 * 2048 + 1024 + (r + 32) * 16) = u32x4{0u, 0u, 0u, 0u};
            } else dv = 4 * (g - 1);
          } else {
            if (g < 2) dv = 12 + 4 * g;
          }
          if (dv >= 0) {
            const int kk = m & 31, s = kk >> 4, k16 = kk & 15;
            const int j = ((k16 >> 3) << 2) | (k16 & 3), h = (k16 >> 2) & 1;
            u16* vp = (u16*)(a.vf + T5 * 2048 + s * 1024 + (dv + 32 * h) * 16 + j * 2);
            vp[0]  = (u16)(pk[0] & 0xffffu);
            vp[8]  = (u16)(pk[0] >> 16);
            vp[16] = (u16)(pk[1] & 0xffffu);
            vp[24] = (u16)(pk[1] >> 16);
          }
        }
      }
    }
    if (NF == 8) {
      const int idx = wm * 64 + lane;
      if (idx < 96) {
        const int tl = idx / 48, rem = idx % 48, s = rem / 24, rem2 = rem % 24, h = rem2 / 12, r = 20 + rem2 % 12;
        const unsigned w = (r == 20) ? 0x3f803f80u : 0u;
        *(u32x4*)(a.vf + ((m0 >> 5) + tl) * 2048 + s * 1024 + (r + 32 * h) * 16) = u32x4{w, w, w, w};
      }
    }
  }
}

__global__ __launch_bounds__(256) void gemm_qkv(GArgs a) {
  extern __shared__ __attribute__((aligned(16))) char smem[];
  if (blockIdx.y == 7) small_body<EPI_QKV, 8>(a, smem);
  else small_body<EPI_QKV, 5>(a, smem);
}
__global__ __launch_bounds__(256) void gemm_res(GArgs a) {
  extern __shared__ __attribute__((aligned(16))) char smem[];
  small_body<EPI_RES, 5>(a, smem);
}

__device__ __forceinline__ float gelu_tanh(float x) {
  const float z = 0.7978845608028654f * (x + 0.044715f * x * x * x);
  const float e = __builtin_amdgcn_exp2f(z * 2.885390081777927f);
  const float t = 1.f - 2.f * __builtin_amdgcn_rcpf(e + 1.f);
  return 0.5f * x * (1.f + t);
}

__global__ __launch_bounds__(512) void gemm_glu(GArgs a) {
  extern __shared__ __attribute__((aligned(16))) char smem[];
  constexpr int BM = 256, NW = 8, NF = 8, NSTAGE = 3;
  constexpr int XI = BM / 8, T = XI + 2 * NF, LH = T / NW;
  constexpr int XBYTES = BM * 128, STAGE = XBYTES + NF * 2048;
  const int tid = threadIdx.x, lane = tid & 63;
  const int wid = __builtin_amdgcn_readfirstlane(tid >> 6);
  const int wm = wid >> 1, wn = wid & 1;
  const int g = lane >> 4;
  const int m0 = blockIdx.x * BM, n0 = blockIdx.y * 64;
  const int K = a.K, nkt = K >> 6;
  float2* ms = (float2*)(smem + NSTAGE * STAGE);
  if (tid < BM) ms[tid] = ln_row(a.st_in, a.ns_in, m0 + tid);

  const char* src[LH];
  unsigned adv[LH];
#pragma unroll
  for (int j = 0; j < LH; ++j) {
    const int i = wid + j * NW;
    if (i < XI) {
      const int P = i * 64 + lane, m = P >> 3, kc = (P & 7) ^ (m & 7);
      src[j] = (const char*)(a.X + (size_t)(m0 + m) * K + kc * 8);
      adv[j] = 128;
    } else {
      const int P = (i - XI) * 64 + lane, b = P >> 4, c = P & 15;
      const int o = b / NF, nf = b - o * NF;
      const int r = (c >> 3) ^ ((o ^ nf) & 1);
      const int k = o * 8 + r * 4 + ((c >> 1) & 3), n = (nf & 3) * 16 + (c & 1) * 8;
      src[j] = (const char*)((nf < 4 ? a.W0 : a.W1) + (size_t)k * DFF + n0 + n);
      adv[j] = 64 * DFF * 2;
    }
  }
  auto issue = [&](int buf) {
#pragma unroll
    for (int j = 0; j < LH; ++j) { glds16(src[j], smem + buf * STAGE + (wid + j * NW) * 1024); src[j] += adv[j]; }
  };

  int xoff[4];
#pragma unroll
  for (int mf = 0; mf < 4; ++mf) {
    const int m = wm * 64 + mf * 16 + (lane & 15);
    xoff[mf] = m * 128 + ((g ^ (m & 7)) << 4);
  }
  const int wbase = XBYTES + (g * NF + 2 * wn) * 256 + ((lane >> 2) & 3) * 32 + (lane & 3) * 8;
  const int wa0 = wbase + (g & 1) * 128, wa1 = wbase + 128 - (g & 1) * 128;

  f32x4 acc[4][4];
#pragma unroll
  for (int i = 0; i < 4; ++i)
#pragma unroll
    for (int mf = 0; mf < 4; ++mf) acc[i][mf] = f32x4{0.f, 0.f, 0.f, 0.f};

  issue(0); issue(1);
  int buf = 0;
  for (int t = 0; t < nkt; ++t) {
    if (t + 1 < nkt) wait_vm<LH>(); else wait_vm<0>();
    raw_barrier();
    int nb = buf + 2; nb = nb >= NSTAGE ? nb - NSTAGE : nb;
    if (t + 2 < nkt) issue(nb);
    const char* sb = smem + buf * STAGE;
#pragma unroll
    for (int ks = 0; ks < 2; ++ks) {
      bf16x8 xf[4];
#pragma unroll
      for (int mf = 0; mf < 4; ++mf) xf[mf] = *(const bf16x8*)(sb + (xoff[mf] ^ (ks * 64)));
#pragma unroll
      for (int i = 0; i < 4; ++i) {
        const int fo = ks * NF * 1024 + ((i < 2) ? i : i + 2) * 256;
        const bf16x8 wf = (i & 1) ? tr_frag(sb + wa1 + fo, sb + wa0 + fo) : tr_frag(sb + wa0 + fo, sb + wa1 + fo);
#pragma unroll
        for (int mf = 0; mf < 4; ++mf) acc[i][mf] = mfma16(wf, xf[mf], acc[i][mf]);
      }
    }
    buf = buf + 1 >= NSTAGE ? 0 : buf + 1;
  }
  __syncthreads();

  float2 mr[4];
#pragma unroll
  for (int mf = 0; mf < 4; ++mf) mr[mf] = ms[wm * 64 + mf * 16 + (lane & 15)];
#pragma unroll
  for (int i = 0; i < 2; ++i) {
    const int n = n0 + (2 * wn + i) * 16 + 4 * g;
    const f32x4 csg = *(const f32x4*)(a.cs0 + n);
    const f32x4 csu = *(const f32x4*)(a.cs1 + n);
#pragma unroll
    for (int mf = 0; mf < 4; ++mf) {
      const int m = m0 + wm * 64 + mf * 16 + (lane & 15);
      float hv[4];
#pragma unroll
      for (int j = 0; j < 4; ++j) {
        const float gb = rbf(mr[mf].y * (acc[i][mf][j] - mr[mf].x * csg[j]));
        const float ub = rbf(mr[mf].y * (acc[i + 2][mf][j] - mr[mf].x * csu[j]));
        hv[j] = rbf(gelu_tanh(gb)) * ub;
      }
      const u32x2 pk = {cvtpk(hv[0], hv[1]), cvtpk(hv[2], hv[3])};
      *(u32x2*)(a.out + (size_t)m * DFF + n) = pk;
    }
  }
}

constexpr float ATHR = 8.f;

__global__ __launch_bounds__(1024) void attn_kernel(const u16* __restrict__ Qs, const char* __restrict__ Kf,
                                                    const char* __restrict__ Vf, u16* __restrict__ Ob) {
  extern __shared__ __attribute__((aligned(16))) char smem[];
  const int tid = threadIdx.x, lane = tid & 63;
  const int wid = __builtin_amdgcn_readfirstlane(tid >> 6);
  const int pl = wid >> 1, kh = wid & 1;
  const int pos = blockIdx.x * 8 + pl;
  const int r = lane & 31, h = lane >> 5;

  const u16* qrow = Qs + (size_t)pos * DM + r * 20;
  const u32x2 qa = *(const u32x2*)(qrow + 8 * h), qb = *(const u32x2*)(qrow + 8 * h + 4);
  const u32x2 qc = *(const u32x2*)(qrow + 16);
  u32x4 q0u = {qa[0], qa[1], qb[0], qb[1]};
  u32x4 q1u = {h ? 0u : qc[0], h ? 0u : qc[1], 0u, 0u};
  const bf16x8 q0 = __builtin_bit_cast(bf16x8, q0u);

  const int tt0 = (wid >> 2) & 3, part = wid & 3;
  const char* gsrc = (part < 2 ? Kf : Vf) + tt0 * 2048 + (part & 1) * 1024 + lane * 16;
  auto issue = [&](int c, int bb) {
    glds16(gsrc + (size_t)c * 4 * 2048, smem + bb * 32768 + wid * 1024);
    glds16(gsrc + (size_t)(32 + c * 4) * 2048, smem + bb * 32768 + (wid + 16) * 1024);
  };

  f32x16 o;
#pragma unroll
  for (int i = 0; i < 16; ++i) o[i] = 0.f;
  float mcur = 0.f;

  issue(0, 0);
  for (int c = 0; c < 8; ++c) {
    if (c + 1 < 8) { issue(c + 1, (c + 1) & 1); wait_vm<2>(); } else wait_vm<0>();
    raw_barrier();
    const char* cb = smem + (c & 1) * 32768 + kh * 16384;
#pragma unroll
    for (int tt = 0; tt < 4; ++tt) {
      const char* tb = cb + tt * 4096 + lane * 16;
      const bf16x8 ka0 = *(const bf16x8*)(tb), ka1 = *(const bf16x8*)(tb + 1024);
      const bf16x8 va0 = *(const bf16x8*)(tb + 2048), va1 = *(const bf16x8*)(tb + 3072);
      f32x16 s;
#pragma unroll
      for (int i = 0; i < 16; ++i) s[i] = 0.f;
      s = mfma32(ka0, q0, s);
      s = mfma32(ka1, __builtin_bit_cast(bf16x8, q1u), s);
      float tmax = __builtin_fmaxf(__builtin_fmaxf(s[0], s[1]), s[2]);
#pragma unroll
      for (int i = 3; i < 15; i += 2) tmax = __builtin_fmaxf(__builtin_fmaxf(tmax, s[i]), s[i + 1]);
      tmax = __builtin_fmaxf(tmax, s[15]);
      const bool first = (c == 0 && tt == 0);
      if (first || __any(tmax > ATHR)) {
        float tmr = __builtin_fmaxf(tmax, __shfl_xor(tmax, 32));
        const float delta = first ? tmr : __builtin_fmaxf(tmr, 0.f);
        const float mnew = rbf(mcur + delta);
        const float de = mnew - mcur;
#pragma unroll
        for (int i = 0; i < 16; ++i) s[i] -= de;
        if (!first) {
          const float sc = __builtin_amdgcn_exp2f(-de);
#pragma unroll
          for (int i = 0; i < 16; ++i) o[i] *= sc;
        }
        mcur = mnew;
        q1u[2] = h ? 0u : (cvtpk(-mnew, 0.f) & 0xffffu);
      }
#pragma unroll
      for (int i = 0; i < 16; ++i) s[i] = __builtin_amdgcn_exp2f(s[i]);
      const u32x4 p0 = {cvtpk(s[0], s[1]), cvtpk(s[2], s[3]), cvtpk(s[4], s[5]), cvtpk(s[6], s[7])};
      const u32x4 p1 = {cvtpk(s[8], s[9]), cvtpk(s[10], s[11]), cvtpk(s[12], s[13]), cvtpk(s[14], s[15])};
      o = mfma32(va0, __builtin_bit_cast(bf16x8, p0), o);
      o = mfma32(va1, __builtin_bit_cast(bf16x8, p1), o);
    }
    raw_barrier();
  }

  __syncthreads();
  float* mg = (float*)smem + pl * (13 * 64);
  if (kh == 1) {
#pragma unroll
    for (int i = 0; i < 12; ++i) mg[i * 64 + lane] = o[i];
    mg[12 * 64 + lane] = mcur;
  }
  __syncthreads();
  if (kh == 1) return;
  {
    const float mb = mg[12 * 64 + lane];
    const float mm = __builtin_fmaxf(mcur, mb);
    const float fa = __builtin_amdgcn_exp2f(mcur - mm), fb = __builtin_amdgcn_exp2f(mb - mm);
#pragma unroll
    for (int i = 0; i < 12; ++i) o[i] = o[i] * fa + mg[i * 64 + lane] * fb;
  }
  const float lsum = __shfl(o[8], r + 32);
  const float inv = 1.f / lsum;
  u16* orow = Ob + (size_t)pos * DM + r * 20 + 4 * h;
  *(u32x2*)(orow)     = u32x2{cvtpk(o[0] * inv, o[1] * inv), cvtpk(o[2] * inv, o[3] * inv)};
  *(u32x2*)(orow + 8) = u32x2{cvtpk(o[4] * inv, o[5] * inv), cvtpk(o[6] * inv, o[7] * inv)};
  if (h == 0) *(u32x2*)(orow + 16) = u32x2{cvtpk(o[8] * inv, o[9] * inv), cvtpk(o[10] * inv, o[11] * inv)};
}

}

extern "C" void kernel_launch(void* const* d_in, const int* in_sizes, int n_in, void* d_out, int out_size,
                              void* d_ws, size_t ws_size, hipStream_t stream) {
  (void)in_sizes; (void)n_in; (void)out_size; (void)ws_size;
  const u16* x_in = (const u16*)d_in[0];
  const u16* wq = (const u16*)d_in[1];
  const u16* wk = (const u16*)d_in[2];
  const u16* wv = (const u16*)d_in[3];
  const u16* wo = (const u16*)d_in[4];
  const u16* wg = (const u16*)d_in[6];
  const u16* wu = (const u16*)d_in[7];
  const u16* wd = (const u16*)d_in[8];
  char* ws = (char*)d_ws;
  u16* xA = (u16*)(ws + WS_XA); u16* xB = (u16*)(ws + WS_XB);
  u16* qs = (u16*)(ws + WS_QS); u16* ob = (u16*)(ws + WS_OB); u16* hb = (u16*)(ws + WS_HB);
  char* kf = ws + WS_KF; char* vf = ws + WS_VF;
  u16* wkv = (u16*)(ws + WS_WKV);
  float* csq = (float*)(ws + WS_CSQ); float* csg = (float*)(ws + WS_CSG); float* csu = (float*)(ws + WS_CSU);
  float2* st0 = (float2*)(ws + WS_ST0); float2* stA = (float2*)(ws + WS_STA); float2* stB = (float2*)(ws + WS_STB);

  constexpr int LDS_QKV = 3 * (64 * 128 + 8 * 2048) + 512;
  constexpr int LDS_RES = 3 * (64 * 128 + 5 * 2048) + 512;
  constexpr int LDS_GLU = 3 * (256 * 128 + 8 * 2048) + 2048;
  constexpr int LDS_ATT = 65536;
  (void)hipFuncSetAttribute((const void*)gemm_qkv, hipFuncAttributeMaxDynamicSharedMemorySize, LDS_QKV);
  (void)hipFuncSetAttribute((const void*)gemm_res, hipFuncAttributeMaxDynamicSharedMemorySize, LDS_RES);
  (void)hipFuncSetAttribute((const void*)gemm_glu, hipFuncAttributeMaxDynamicSharedMemorySize, LDS_GLU);
  (void)hipFuncSetAttribute((const void*)attn_kernel, hipFuncAttributeMaxDynamicSharedMemorySize, LDS_ATT);

  hipLaunchKernelGGL(k0_prep, dim3(512 + 18), dim3(256), 0, stream, x_in, wq, wk, wv, wg, wu, st0, csq, csg, csu, wkv);

  for (int layer = 0; layer < 2; ++layer) {
    const u16* xin = layer == 0 ? x_in : xB;
    GArgs a;
    a.X = xin; a.W0 = wq; a.W1 = wkv; a.res = nullptr; a.out = qs;
    a.st_in = layer == 0 ? st0 : stB; a.st_out = nullptr; a.cs0 = csq; a.cs1 = nullptr;
    a.kf = kf; a.vf = vf; a.K = DM; a.ns_in = layer == 0 ? 1 : 8;
    hipLaunchKernelGGL(gemm_qkv, dim3(32, 8), dim3(256), LDS_QKV, stream, a);
    hipLaunchKernelGGL(attn_kernel, dim3(256), dim3(1024), LDS_ATT, stream, (const u16*)qs, (const char*)kf, (const char*)vf, ob);
    a.X = ob; a.W0 = wo; a.W1 = nullptr; a.res = xin; a.out = xA; a.st_in = nullptr; a.st_out = stA;
    a.cs0 = nullptr; a.cs1 = nullptr; a.kf = nullptr; a.vf = nullptr; a.K = DM; a.ns_in = 0;
    hipLaunchKernelGGL(gemm_res, dim3(32, 8), dim3(256), LDS_RES, stream, a);
    a.X = xA; a.W0 = wg; a.W1 = wu; a.res = nullptr; a.out = hb; a.st_in = stA; a.st_out = nullptr;
    a.cs0 = csg; a.cs1 = csu; a.K = DM; a.ns_in = 8;
    hipLaunchKernelGGL(gemm_glu, dim3(8, 27), dim3(512), LDS_GLU, stream, a);
    a.X = hb; a.W0 = wd; a.W1 = nullptr; a.res = xA; a.out = layer == 0 ? xB : (u16*)d_out;
    a.st_in = nullptr; a.st_out = stB; a.cs0 = nullptr; a.cs1 = nullptr; a.K = DFF; a.ns_in = 0;
    hipLaunchKernelGGL(gemm_res, dim3(32, 8), dim3(256), LDS_RES, stream, a);
  }
}
```

```cpp
#include <hip/hip_runtime.h>
#include <hip/hip_bf16.h>
#include <stdint.h>

typedef short    bf16x8 __attribute__((ext_vector_type(8)));
typedef short    s16x4  __attribute__((ext_vector_type(4)));
typedef float    f32x4  __attribute__((ext_vector_type(4)));
typedef float    f32x16 __attribute__((ext_vector_type(16)));
typedef unsigned u32x4  __attribute__((ext_vector_type(4)));
typedef unsigned u32x2  __attribute__((ext_vector_type(2)));
typedef unsigned short u16;

namespace {

constexpr int SEQ = 2048, DM = 640, DFF = 1728;
constexpr float QSCALE = 0.22360679774997896f * 1.4426950408889634f;
constexpr float LN_EPS = 1e-5f;

constexpr size_t ALN(size_t x) { return (x + 255) / 256 * 256; }
constexpr size_t WS_XA  = 0;
constexpr size_t WS_XB  = WS_XA + ALN((size_t)SEQ * DM * 2);
constexpr size_t WS_QS  = WS_XB + ALN((size_t)SEQ * DM * 2);
constexpr size_t WS_OB  = WS_QS + ALN((size_t)SEQ * DM * 2);
constexpr size_t WS_HB  = WS_OB + ALN((size_t)SEQ * DM * 2);
constexpr size_t WS_KF  = WS_HB + ALN((size_t)SEQ * DFF * 2);
constexpr size_t WS_VF  = WS_KF + 131072;
constexpr size_t WS_WKV = WS_VF + 131072;
constexpr size_t WS_CSQ = WS_WKV + ALN(640 * 48 * 2);
constexpr size_t WS_CSG = WS_CSQ + ALN((688 + 480) * 4);
constexpr size_t WS_CSU = WS_CSG + ALN(DFF * 4);
constexpr size_t WS_ST0 = WS_CSU + ALN(DFF * 4);
constexpr size_t WS_STA = WS_ST0 + ALN(SEQ * 8);
constexpr size_t WS_STB = WS_STA + ALN(8 * SEQ * 8);

__device__ __forceinline__ float bf2f(unsigned v) { return __uint_as_float(v << 16); }
__device__ __forceinline__ float bfhi(unsigned v) { return __uint_as_float(v & 0xffff0000u); }
__device__ __forceinline__ unsigned cvtpk(float lo, float hi) {
  unsigned r; asm("v_cvt_pk_bf16_f32 %0, %1, %2" : "=v"(r) : "v"(lo), "v"(hi)); return r;
}
__device__ __forceinline__ float rbf(float x) { return __uint_as_float(cvtpk(0.f, x)); }

template <int N> __device__ __forceinline__ void wait_vm() { asm volatile("s_waitcnt vmcnt(%0)" :: "i"(N) : "memory"); }
__device__ __forceinline__ void raw_barrier() {
  asm volatile("" ::: "memory"); __builtin_amdgcn_s_barrier(); asm volatile("" ::: "memory");
}
__device__ __forceinline__ void lds_barrier() {
  asm volatile("s_waitcnt lgkmcnt(0)" ::: "memory"); __builtin_amdgcn_s_barrier(); asm volatile("" ::: "memory");
}
__device__ __forceinline__ void glds16(const void* g, char* lds) {
#if defined(__HIP_DEVICE_COMPILE__)
  const unsigned dst = __builtin_amdgcn_readfirstlane((unsigned)(size_t)(__attribute__((address_space(3))) char*)lds);
  unsigned keep;
  asm volatile("s_mov_b32 %0, m0\n\ts_mov_b32 m0, %2\n\ts_nop 0\n\tglobal_load_lds_dwordx4 %1, off\n\ts_mov_b32 m0, %0"
               : "=&s"(keep) : "v"(g), "s"(dst) : "memory");
#endif
}
__device__ __forceinline__ bf16x8 tr_frag(const char* p0, const char* p1) {
  bf16x8 r = {};
#if defined(__HIP_DEVICE_COMPILE__)
  s16x4 lo = __builtin_amdgcn_ds_read_tr16_b64_v4i16((__attribute__((address_space(3))) s16x4*)p0);
  s16x4 hi = __builtin_amdgcn_ds_read_tr16_b64_v4i16((__attribute__((address_space(3))) s16x4*)p1);
  r = __builtin_shufflevector(lo, hi, 0, 1, 2, 3, 4, 5, 6, 7);
#endif
  return r;
}
__device__ __forceinline__ f32x4 mfma16(bf16x8 a, bf16x8 b, f32x4 c) {
  return __builtin_amdgcn_mfma_f32_16x16x32_bf16(a, b, c, 0, 0, 0);
}
__device__ __forceinline__ f32x16 mfma32(bf16x8 a, bf16x8 b, f32x16 c) {
  return __builtin_amdgcn_mfma_f32_32x32x16_bf16(a, b, c, 0, 0, 0);
}
template <int LH, int C> __device__ __forceinline__ void wait_steady(int L) {
  if (L == LH) wait_vm<LH * C>();
  else wait_vm<(LH - 1) * C>();
}
template <int LH, int CMAX> __device__ __forceinline__ void wait_rem(int L, int rem) {
  if (rem >= CMAX) wait_steady<LH, CMAX>(L);
  else if constexpr (CMAX > 0) wait_rem<LH, CMAX - 1>(L, rem);
}

__global__ __launch_bounds__(256) void k0_prep(const u16* __restrict__ x, const u16* __restrict__ wq,
                                               const u16* __restrict__ wk, const u16* __restrict__ wv,
                                               const u16* __restrict__ wg, const u16* __restrict__ wu,
                                               float2* __restrict__ st0, float* __restrict__ csq,
                                               float* __restrict__ csg, float* __restrict__ csu,
                                               u16* __restrict__ wkv) {
  __shared__ float red[64][65];
  int b = blockIdx.x;
  const int t = threadIdx.x;
  if (b < 512) {
    const int row = b * 4 + (t >> 6), l = t & 63;
    const unsigned* xr = (const unsigned*)(x + (size_t)row * DM);
    float s = 0.f, q = 0.f;
#pragma unroll
    for (int j = 0; j < 5; ++j) {
      const unsigned v = xr[j * 64 + l];
      const float a = bf2f(v), c = bfhi(v);
      s += a + c; q += a * a + c * c;
    }
#pragma unroll
    for (int o = 32; o > 0; o >>= 1) { s += __shfl_xor(s, o); q += __shfl_xor(q, o); }
    if (l == 0) st0[row] = make_float2(s, q);
    return;
  }
  b -= 512;
  if (b < 64) {
    const u16* W; int N; float* out; int cb;
    if (b < 10)      { W = wq; N = DM;  out = csq; cb = b; }
    else if (b < 37) { W = wg; N = DFF; out = csg; cb = b - 10; }
    else             { W = wu; N = DFF; out = csu; cb = b - 37; }
    const int cg = t & 7, rg = t >> 3, col = cb * 64 + cg * 8;
    float a[8];
#pragma unroll
    for (int j = 0; j < 8; ++j) a[j] = 0.f;
#pragma unroll 5
    for (int i = 0; i < 20; ++i) {
      const int r = i * 32 + rg;
      const u32x4 v = *(const u32x4*)(W + (size_t)r * N + col);
#pragma unroll
      for (int j = 0; j < 4; ++j) { a[2 * j] += bf2f(v[j]); a[2 * j + 1] += bfhi(v[j]); }
    }
#pragma unroll
    for (int j = 0; j < 8; ++j) red[rg][cg * 8 + j] = a[j];
    __syncthreads();
    if (t < 64) {
      float s = 0.f;
#pragma unroll
      for (int r = 0; r < 32; ++r) s += red[r][t];
      out[cb * 64 + t] = s;
    }
    return;
  }
  b -= 64;
  if (b < 10) {
    const int k = b * 64 + (t >> 2), part = t & 3;
    float* part_out = csq + 688 + b * 48;
    u16 v[12];
#pragma unroll
    for (int j = 0; j < 12; ++j) {
      const int c = part * 12 + j;
      v[j] = c < 20 ? wk[k * 20 + c] : (c < 40 ? wv[k * 20 + c - 20] : (u16)0);
    }
#pragma unroll
    for (int j = 0; j < 12; ++j) { wkv[k * 48 + part * 12 + j] = v[j]; }
#pragma unroll
    for (int j = 0; j < 12; ++j) red[t >> 2][part * 12 + j] = bf2f(v[j]);
    __syncthreads();
    if (t < 48) {
      float s = 0.f;
#pragma unroll 8
      for (int r = 0; r < 64; ++r) s += red[r][t];
      part_out[t] = s;
    }
  }
}

struct GArgs {
  const u16* X;
  const u16* W0;
  const u16* W1;
  const u16* res;
  u16* out;
  const float2* st_in;
  float2* st_out;
  const float* cs0;
  const float* cs1;
  char* kf;
  char* vf;
  int K;
  int ns_in;
};

enum { EPI_QKV = 0, EPI_RES = 1 };
constexpr int SMALL_NSTAGE = 6;

__device__ __forceinline__ float2 ln_row(const float2* st, int ns, int row) {
  float s = 0.f, q = 0.f;
  for (int i = 0; i < ns; ++i) { const float2 v = st[(size_t)i * SEQ + row]; s += v.x; q += v.y; }
  const float mean = s * (1.f / DM);
  float var = q * (1.f / DM) - mean * mean;
  var = var < 0.f ? 0.f : var;
  return make_float2(mean, __builtin_amdgcn_rsqf(var + LN_EPS));
}

template <int NF> __device__ __forceinline__ void w_chunk(int P, int& k, int& nc) {
  if (NF == 8) {
    k = P >> 4;
    nc = (P & 15) ^ (2 * ((k & 3) + 4 * ((k >> 3) & 1)));
  } else {
    const int rho = P / (2 * NF);
    nc = P - rho * (2 * NF);
    k = (rho & ~7) | ((rho ^ (((rho >> 3) & 1) << 2)) & 7);
  }
}

template <int EPI, int NF>
__device__ __forceinline__ void small_body(const GArgs& a, char* smem) {
  constexpr int BM = 64, NP = 4, NSTAGE = SMALL_NSTAGE;
  constexpr int XI = BM / 8, T = XI + 2 * NF, LH = (T + NP - 1) / NP;
  constexpr int XBYTES = BM * 128, STAGE = XBYTES + NF * 2048;
  const int tid = threadIdx.x, lane = tid & 63;
  const int wid = __builtin_amdgcn_readfirstlane(tid >> 6);
  const int m0 = blockIdx.x * BM, n0 = blockIdx.y * 80;
  const int K = a.K, nkt = K >> 6;
  float2* ms = (float2*)(smem + NSTAGE * STAGE);

  if (wid >= 4) {
    const int pw = wid - 4;
    const int L = (T - pw + NP - 1) / NP;
    const char* src[LH];
    unsigned adv[LH];
#pragma unroll
    for (int j = 0; j < LH; ++j) {
      const int i = pw + j * NP;
      src[j] = nullptr; adv[j] = 0;
      if (i < XI) {
        const int P = i * 64 + lane, m = P >> 3, kc = (P & 7) ^ (m & 7);
        src[j] = (const char*)(a.X + (size_t)(m0 + m) * K + kc * 8);
        adv[j] = 128;
      } else if (i < T) {
        int k, nc;
        w_chunk<NF>((i - XI) * 64 + lane, k, nc);
        if (EPI == EPI_QKV && nc >= 10) {
          src[j] = (const char*)(a.W1 + (size_t)k * 48 + (nc - 10) * 8);
          adv[j] = 64 * 48 * 2;
        } else {
          src[j] = (const char*)(a.W0 + (size_t)k * DM + n0 + nc * 8);
          adv[j] = 64 * DM * 2;
        }
      }
    }
    auto issue = [&](int buf) {
#pragma unroll
      for (int j = 0; j < LH; ++j)
        if (j < L) { glds16(src[j], smem + buf * STAGE + (pw + j * NP) * 1024); src[j] += adv[j]; }
    };
#pragma unroll
    for (int s = 0; s < NSTAGE - 1; ++s) if (s < nkt) issue(s);
    int nb = NSTAGE - 1;
    for (int t = 0; t < nkt; ++t) {
      wait_rem<LH, NSTAGE - 2>(L, nkt - 1 - t);
      raw_barrier();
      if (t + NSTAGE - 1 < nkt) issue(nb);
      nb = nb + 1 >= NSTAGE ? 0 : nb + 1;
    }
    raw_barrier();
    raw_barrier();
    return;
  }

  const int wm = wid & 1, wk = wid >> 1;
  const int g = lane >> 4;
  if (EPI == EPI_QKV) {
    if (tid < BM) ms[tid] = ln_row(a.st_in, a.ns_in, m0 + tid);
  }
  int xoff[2];
#pragma unroll
  for (int mf = 0; mf < 2; ++mf) {
    const int m = wm * 32 + mf * 16 + (lane & 15);
    xoff[mf] = m * 128 + (((4 * wk + g) ^ (m & 7)) << 4);
  }
  int woff[NF];
  {
    const int o = 4 * wk + g, q = (lane >> 2) & 3, p = lane & 3;
#pragma unroll
    for (int nf = 0; nf < NF; ++nf) {
      if (NF == 8) woff[nf] = XBYTES + (8 * o + q) * 256 + ((nf ^ (q + 4 * (o & 1))) << 5) + p * 8;
      else woff[nf] = XBYTES + (8 * o + 4 * (o & 1) + q) * (NF * 32) + nf * 32 + p * 8;
    }
  }
  const int wr1 = (NF == 8) ? 1024 : (4 - 8 * (g & 1)) * (NF * 32);

  f32x4 acc[NF][2];
#pragma unroll
  for (int nf = 0; nf < NF; ++nf) { acc[nf][0] = f32x4{0.f, 0.f, 0.f, 0.f}; acc[nf][1] = acc[nf][0]; }

  int buf = 0;
  for (int t = 0; t < nkt; ++t) {
    raw_barrier();
    const char* sb = smem + buf * STAGE;
    bf16x8 xf[2];
#pragma unroll
    for (int mf = 0; mf < 2; ++mf) xf[mf] = *(const bf16x8*)(sb + xoff[mf]);
#pragma unroll
    for (int nf = 0; nf < NF; ++nf) {
      const bf16x8 wf = tr_frag(sb + woff[nf], sb + woff[nf] + wr1);
#pragma unroll
      for (int mf = 0; mf < 2; ++mf) acc[nf][mf] = mfma16(wf, xf[mf], acc[nf][mf]);
    }
    buf = buf + 1 >= NSTAGE ? 0 : buf + 1;
  }

  u32x2 xo[NF][2];
  f32x4 csv[5];
  if (wk == 0) {
    if (EPI == EPI_RES) {
#pragma unroll
      for (int nf = 0; nf < NF; ++nf)
#pragma unroll
        for (int mf = 0; mf < 2; ++mf)
          xo[nf][mf] = *(const u32x2*)(a.res + (size_t)(m0 + wm * 32 + mf * 16 + (lane & 15)) * DM + n0 + nf * 16 + 4 * g);
    } else {
#pragma unroll
      for (int nf = 0; nf < 5; ++nf) csv[nf] = *(const f32x4*)(a.cs0 + n0 + nf * 16 + 4 * g);
    }
  }
  lds_barrier();
  float* red = (float*)smem;
  if (wk == 1) {
#pragma unroll
    for (int nf = 0; nf < NF; ++nf)
#pragma unroll
      for (int mf = 0; mf < 2; ++mf)
        *(f32x4*)(red + (((wm * NF + nf) * 2 + mf) * 64 + lane) * 4) = acc[nf][mf];
  }
  lds_barrier();
  if (wk == 1) return;
#pragma unroll
  for (int nf = 0; nf < NF; ++nf)
#pragma unroll
    for (int mf = 0; mf < 2; ++mf)
      acc[nf][mf] += *(const f32x4*)(red + (((wm * NF + nf) * 2 + mf) * 64 + lane) * 4);

  if (EPI == EPI_RES) {
    float ssum[2] = {0.f, 0.f}, ssq[2] = {0.f, 0.f};
#pragma unroll
    for (int nf = 0; nf < NF; ++nf) {
      const int n = n0 + nf * 16 + 4 * g;
#pragma unroll
      for (int mf = 0; mf < 2; ++mf) {
        const int m = m0 + wm * 32 + mf * 16 + (lane & 15);
        const u32x2 xr = xo[nf][mf];
        const float v0 = bf2f(xr[0]) + rbf(acc[nf][mf][0]);
        const float v1 = bfhi(xr[0]) + rbf(acc[nf][mf][1]);
        const float v2 = bf2f(xr[1]) + rbf(acc[nf][mf][2]);
        const float v3 = bfhi(xr[1]) + rbf(acc[nf][mf][3]);
        const u32x2 pk = {cvtpk(v0, v1), cvtpk(v2, v3)};
        *(u32x2*)(a.out + (size_t)m * DM + n) = pk;
        const float f0 = bf2f(pk[0]), f1 = bfhi(pk[0]), f2 = bf2f(pk[1]), f3 = bfhi(pk[1]);
        ssum[mf] += (f0 + f1) + (f2 + f3);
        ssq[mf] += (f0 * f0 + f1 * f1) + (f2 * f2 + f3 * f3);
      }
    }
#pragma unroll
    for (int mf = 0; mf < 2; ++mf) {
      float s = ssum[mf], q = ssq[mf];
      s += __shfl_xor(s, 16); q += __shfl_xor(q, 16);
      s += __shfl_xor(s, 32); q += __shfl_xor(q, 32);
      if (lane < 16) a.st_out[(size_t)blockIdx.y * SEQ + m0 + wm * 32 + mf * 16 + lane] = make_float2(s, q);
    }
  } else {
    float2 mr[2];
#pragma unroll
    for (int mf = 0; mf < 2; ++mf) mr[mf] = ms[wm * 32 + mf * 16 + (lane & 15)];
#pragma unroll
    for (int nf = 0; nf < NF; ++nf) {
      if (nf < 5) {
        const int n = n0 + nf * 16 + 4 * g;
        const f32x4 cs = csv[nf];
#pragma unroll
        for (int mf = 0; mf < 2; ++mf) {
          const int m = m0 + wm * 32 + mf * 16 + (lane & 15);
          const float sc = mr[mf].y * QSCALE;
          float v[4];
#pragma unroll
          for (int j = 0; j < 4; ++j) v[j] = sc * (acc[nf][mf][j] - mr[mf].x * cs[j]);
          const u32x2 pk = {cvtpk(v[0], v[1]), cvtpk(v[2], v[3])};
          *(u32x2*)(a.out + (size_t)m * DM + n) = pk;
        }
      } else {
        const int kvf = nf - 5;
        f32x4 cs = f32x4{0.f, 0.f, 0.f, 0.f};
#pragma unroll
        for (int pb = 0; pb < 10; ++pb) cs += *(const f32x4*)(a.cs0 + 688 + pb * 48 + kvf * 16 + 4 * g);
#pragma unroll
        for (int mf = 0; mf < 2; ++mf) {
          const int m = m0 + wm * 32 + mf * 16 + (lane & 15);
          float v[4];
#pragma unroll
          for (int j = 0; j < 4; ++j) v[j] = mr[mf].y * (acc[nf][mf][j] - mr[mf].x * cs[j]);
          const u32x2 pk = {cvtpk(v[0], v[1]), cvtpk(v[2], v[3])};
          const int T5 = m >> 5, r = m & 31;
          int dv = -1;
          if (kvf == 0) {
            *(u32x2*)(a.kf + T5 * 2048 + (r + 32 * (g >> 1)) * 16 + (g & 1) * 8) = pk;
          } else if (kvf == 1) {
            if (g == 0) {
              const u32x4 w = {pk[0], pk[1], 0x3f80u, 0u};
              *(u32x4*)(a.kf + T5 * 2048 + 1024 + r * 16) = w;
              *(u32x4*)(a.kf + T5 * 2048 + 1024 + (r + 32) * 16) = u32x4{0u, 0u, 0u, 0u};
            } else dv = 4 * (g - 1);
          } else {
            if (g < 2) dv = 12 + 4 * g;
          }
          if (dv >= 0) {
            const int kk = m & 31, s = kk >> 4, k16 = kk & 15;
            const int j = ((k16 >> 3) << 2) | (k16 & 3), h = (k16 >> 2) & 1;
            u16* vp = (u16*)(a.vf + T5 * 2048 + s * 1024 + (dv + 32 * h) * 16 + j * 2);
            vp[0]  = (u16)(pk[0] & 0xffffu);
            vp[8]  = (u16)(pk[0] >> 16);
            vp[16] = (u16)(pk[1] & 0xffffu);
            vp[24] = (u16)(pk[1] >> 16);
          }
        }
      }
    }
    if (NF == 8) {
      const int idx = wm * 64 + lane;
      if (idx < 96) {
        const int tl = idx / 48, rem = idx % 48, s = rem / 24, rem2 = rem % 24, h = rem2 / 12, r = 20 + rem2 % 12;
        const unsigned w = (r == 20) ? 0x3f803f80u : 0u;
        *(u32x4*)(a.vf + ((m0 >> 5) + tl) * 2048 + s * 1024 + (r + 32 * h) * 16) = u32x4{w, w, w, w};
      }
    }
  }
}

__global__ __launch_bounds__(512) void gemm_qkv(GArgs a) {
  extern __shared__ __attribute__((aligned(16))) char smem[];
  if (blockIdx.y == 7) small_body<EPI_QKV, 8>(a, smem);
  else small_body<EPI_QKV, 5>(a, smem);
}
__global__ __launch_bounds__(512) void gemm_res(GArgs a) {
  extern __shared__ __attribute__((aligned(16))) char smem[];
  small_body<EPI_RES, 5>(a, smem);
}

__device__ __forceinline__ float gelu_tanh(float x) {
  const float z = 0.7978845608028654f * (x + 0.044715f * x * x * x);
  const float e = __builtin_amdgcn_exp2f(z * 2.885390081777927f);
  const float t = 1.f - 2.f * __builtin_amdgcn_rcpf(e + 1.f);
  return 0.5f * x * (1.f + t);
}

__global__ __launch_bounds__(768) void gemm_glu(GArgs a) {
  extern __shared__ __attribute__((aligned(16))) char smem[];
  constexpr int BM = 256, NP = 4, NF = 8, NSTAGE = 3;
  constexpr int XI = BM / 8, T = XI + 2 * NF, LH = T / NP;
  constexpr int XBYTES = BM * 128, STAGE = XBYTES + NF * 2048;
  const int tid = threadIdx.x, lane = tid & 63;
  const int wid = __builtin_amdgcn_readfirstlane(tid >> 6);
  const int m0 = blockIdx.x * BM, n0 = blockIdx.y * 64;
  const int K = a.K, nkt = K >> 6;
  float2* ms = (float2*)(smem + NSTAGE * STAGE);

  if (wid >= 8) {
    const int pw = wid - 8;
    const char* src[LH];
    unsigned adv[LH];
#pragma unroll
    for (int j = 0; j < LH; ++j) {
      const int i = pw + j * NP;
      if (i < XI) {
        const int P = i * 64 + lane, m = P >> 3, kc = (P & 7) ^ (m & 7);
        src[j] = (const char*)(a.X + (size_t)(m0 + m) * K + kc * 8);
        adv[j] = 128;
      } else {
        int k, nc;
        w_chunk<8>((i - XI) * 64 + lane, k, nc);
        src[j] = (const char*)((nc < 8 ? a.W0 : a.W1) + (size_t)k * DFF + n0 + (nc & 7) * 8);
        adv[j] = 64 * DFF * 2;
      }
    }
    auto issue = [&](int buf) {
#pragma unroll
      for (int j = 0; j < LH; ++j) { glds16(src[j], smem + buf * STAGE + (pw + j * NP) * 1024); src[j] += adv[j]; }
    };
#pragma unroll
    for (int s = 0; s < NSTAGE - 1; ++s) if (s < nkt) issue(s);
    int nb = NSTAGE - 1;
    for (int t = 0; t < nkt; ++t) {
      wait_rem<LH, NSTAGE - 2>(LH, nkt - 1 - t);
      raw_barrier();
      if (t + NSTAGE - 1 < nkt) issue(nb);
      nb = nb + 1 >= NSTAGE ? 0 : nb + 1;
    }
    raw_barrier();
    return;
  }

  const int wm = wid >> 1, wn = wid & 1;
  const int g = lane >> 4;
  if (tid < BM) ms[tid] = ln_row(a.st_in, a.ns_in, m0 + tid);

  int xoff[4];
#pragma unroll
  for (int mf = 0; mf < 4; ++mf) {
    const int m = wm * 64 + mf * 16 + (lane & 15);
    xoff[mf] = m * 128 + ((g ^ (m & 7)) << 4);
  }
  int woff[4];
  {
    const int q = (lane >> 2) & 3, p = lane & 3;
#pragma unroll
    for (int i = 0; i < 4; ++i) {
      const int nf = 2 * wn + ((i < 2) ? i : i + 2);
      woff[i] = XBYTES + (8 * g + q) * 256 + ((nf ^ (q + 4 * (g & 1))) << 5) + p * 8;
    }
  }

  f32x4 acc[4][4];
#pragma unroll
  for (int i = 0; i < 4; ++i)
#pragma unroll
    for (int mf = 0; mf < 4; ++mf) acc[i][mf] = f32x4{0.f, 0.f, 0.f, 0.f};

  int buf = 0;
  for (int t = 0; t < nkt; ++t) {
    raw_barrier();
    const char* sb = smem + buf * STAGE;
#pragma unroll
    for (int ks = 0; ks < 2; ++ks) {
      bf16x8 xf[4];
#pragma unroll
      for (int mf = 0; mf < 4; ++mf) xf[mf] = *(const bf16x8*)(sb + (xoff[mf] ^ (ks * 64)));
#pragma unroll
      for (int i = 0; i < 4; ++i) {
        const bf16x8 wf = tr_frag(sb + woff[i] + ks * 8192, sb + woff[i] + ks * 8192 + 1024);
#pragma unroll
        for (int mf = 0; mf < 4; ++mf) acc[i][mf] = mfma16(wf, xf[mf], acc[i][mf]);
      }
    }
    buf = buf + 1 >= NSTAGE ? 0 : buf + 1;
  }
  f32x4 csgv[2], csuv[2];
#pragma unroll
  for (int i = 0; i < 2; ++i) {
    csgv[i] = *(const f32x4*)(a.cs0 + n0 + (2 * wn + i) * 16 + 4 * g);
    csuv[i] = *(const f32x4*)(a.cs1 + n0 + (2 * wn + i) * 16 + 4 * g);
  }
  lds_barrier();

  float2 mr[4];
#pragma unroll
  for (int mf = 0; mf < 4; ++mf) mr[mf] = ms[wm * 64 + mf * 16 + (lane & 15)];
#pragma unroll
  for (int i = 0; i < 2; ++i) {
    const int n = n0 + (2 * wn + i) * 16 + 4 * g;
    const f32x4 csg = csgv[i];
    const f32x4 csu = csuv[i];
#pragma unroll
    for (int mf = 0; mf < 4; ++mf) {
      const int m = m0 + wm * 64 + mf * 16 + (lane & 15);
      float hv[4];
#pragma unroll
      for (int j = 0; j < 4; ++j) {
        const float gb = rbf(mr[mf].y * (acc[i][mf][j] - mr[mf].x * csg[j]));
        const float ub = rbf(mr[mf].y * (acc[i + 2][mf][j] - mr[mf].x * csu[j]));
        hv[j] = rbf(gelu_tanh(gb)) * ub;
      }
      const u32x2 pk = {cvtpk(hv[0], hv[1]), cvtpk(hv[2], hv[3])};
      *(u32x2*)(a.out + (size_t)m * DFF + n) = pk;
    }
  }
}

constexpr float ATHR = 8.f;

__global__ __launch_bounds__(1024) void attn_kernel(const u16* __restrict__ Qs, const char* __restrict__ Kf,
                                                    const char* __restrict__ Vf, u16* __restrict__ Ob) {
  extern __shared__ __attribute__((aligned(16))) char smem[];
  const int tid = threadIdx.x, lane = tid & 63;
  const int wid = __builtin_amdgcn_readfirstlane(tid >> 6);
  const int pl = wid >> 1, kh = wid & 1;
  const int pos = blockIdx.x * 8 + pl;
  const int r = lane & 31, h = lane >> 5;

  const u16* qrow = Qs + (size_t)pos * DM + r * 20;
  const u32x2 qa = *(const u32x2*)(qrow + 8 * h), qb = *(const u32x2*)(qrow + 8 * h + 4);
  const u32x2 qc = *(const u32x2*)(qrow + 16);
  u32x4 q0u = {qa[0], qa[1], qb[0], qb[1]};
  u32x4 q1u = {h ? 0u : qc[0], h ? 0u : qc[1], 0u, 0u};
  const bf16x8 q0 = __builtin_bit_cast(bf16x8, q0u);

  const int tt0 = (wid >> 2) & 3, part = wid & 3;
  const char* gsrc = (part < 2 ? Kf : Vf) + tt0 * 2048 + (part & 1) * 1024 + lane * 16;
  auto issue = [&](int c, int bb) {
    glds16(gsrc + (size_t)c * 4 * 2048, smem + bb * 32768 + wid * 1024);
    glds16(gsrc + (size_t)(32 + c * 4) * 2048, smem + bb * 32768 + (wid + 16) * 1024);
  };
  auto qk = [&](const char* tb) {
    const bf16x8 ka0 = *(const bf16x8*)(tb), ka1 = *(const bf16x8*)(tb + 1024);
    f32x16 s;
#pragma unroll
    for (int i = 0; i < 16; ++i) s[i] = 0.f;
    s = mfma32(ka0, q0, s);
    s = mfma32(ka1, __builtin_bit_cast(bf16x8, q1u), s);
    return s;
  };

  f32x16 o;
#pragma unroll
  for (int i = 0; i < 16; ++i) o[i] = 0.f;
  float mcur = 0.f;

  issue(0, 0);
  for (int c = 0; c < 8; ++c) {
    if (c + 1 < 8) { issue(c + 1, (c + 1) & 1); wait_vm<2>(); } else wait_vm<0>();
    raw_barrier();
    const char* cb = smem + (c & 1) * 32768 + kh * 16384 + lane * 16;
#pragma unroll
    for (int tt = 0; tt < 4; ++tt) {
      const char* tb = cb + tt * 4096;
      f32x16 s = qk(tb);
      const bf16x8 va0 = *(const bf16x8*)(tb + 2048), va1 = *(const bf16x8*)(tb + 3072);
      float tmax = __builtin_fmaxf(__builtin_fmaxf(s[0], s[1]), s[2]);
#pragma unroll
      for (int i = 3; i < 15; i += 2) tmax = __builtin_fmaxf(__builtin_fmaxf(tmax, s[i]), s[i + 1]);
      tmax = __builtin_fmaxf(tmax, s[15]);
      const bool first = (c == 0 && tt == 0);
      if (first || __any(tmax > ATHR)) {
        const float tmr = __builtin_fmaxf(tmax, __shfl_xor(tmax, 32));
        const float delta = first ? tmr : __builtin_fmaxf(tmr, 0.f);
        const float mnew = rbf(mcur + delta);
        const float de = mnew - mcur;
#pragma unroll
        for (int i = 0; i < 16; ++i) s[i] -= de;
        if (!first) {
          const float sc = __builtin_amdgcn_exp2f(-de);
#pragma unroll
          for (int i = 0; i < 16; ++i) o[i] *= sc;
        }
        mcur = mnew;
        q1u[2] = h ? 0u : (cvtpk(-mnew, 0.f) & 0xffffu);
      }
#pragma unroll
      for (int i = 0; i < 16; ++i) s[i] = __builtin_amdgcn_exp2f(s[i]);
      const u32x4 p0 = {cvtpk(s[0], s[1]), cvtpk(s[2], s[3]), cvtpk(s[4], s[5]), cvtpk(s[6], s[7])};
      const u32x4 p1 = {cvtpk(s[8], s[9]), cvtpk(s[10], s[11]), cvtpk(s[12], s[13]), cvtpk(s[14], s[15])};
      o = mfma32(va0, __builtin_bit_cast(bf16x8, p0), o);
      o = mfma32(va1, __builtin_bit_cast(bf16x8, p1), o);
    }
    raw_barrier();
  }

  __syncthreads();
  float* mg = (float*)smem + pl * (13 * 64);
  if (kh == 1) {
#pragma unroll
    for (int i = 0; i < 12; ++i) mg[i * 64 + lane] = o[i];
    mg[12 * 64 + lane] = mcur;
  }
  __syncthreads();
  if (kh == 1) return;
  {
    const float mb = mg[12 * 64 + lane];
    const float mm = __builtin_fmaxf(mcur, mb);
    const float fa = __builtin_amdgcn_exp2f(mcur - mm), fb = __builtin_amdgcn_exp2f(mb - mm);
#pragma unroll
    for (int i = 0; i < 12; ++i) o[i] = o[i] * fa + mg[i * 64 + lane] * fb;
  }
  const float lsum = __shfl(o[8], r + 32);
  const float inv = 1.f / lsum;
  u16* orow = Ob + (size_t)pos * DM + r * 20 + 4 * h;
  *(u32x2*)(orow)     = u32x2{cvtpk(o[0] * inv, o[1] * inv), cvtpk(o[2] * inv, o[3] * inv)};
  *(u32x2*)(orow + 8) = u32x2{cvtpk(o[4] * inv, o[5] * inv), cvtpk(o[6] * inv, o[7] * inv)};
  if (h == 0) *(u32x2*)(orow + 16) = u32x2{cvtpk(o[8] * inv, o[9] * inv), cvtpk(o[10] * inv, o[11] * inv)};
}

}

extern "C" void kernel_launch(void* const* d_in, const int* in_sizes, int n_in, void* d_out, int out_size,
                              void* d_ws, size_t ws_size, hipStream_t stream) {
  (void)in_sizes; (void)n_in; (void)out_size; (void)ws_size;
  const u16* x_in = (const u16*)d_in[0];
  const u16* wq = (const u16*)d_in[1];
  const u16* wk = (const u16*)d_in[2];
  const u16* wv = (const u16*)d_in[3];
  const u16* wo = (const u16*)d_in[4];
  const u16* wg = (const u16*)d_in[6];
  const u16* wu = (const u16*)d_in[7];
  const u16* wd = (const u16*)d_in[8];
  char* ws = (char*)d_ws;
  u16* xA = (u16*)(ws + WS_XA); u16* xB = (u16*)(ws + WS_XB);
  u16* qs = (u16*)(ws + WS_QS); u16* ob = (u16*)(ws + WS_OB); u16* hb = (u16*)(ws + WS_HB);
  char* kf = ws + WS_KF; char* vf = ws + WS_VF;
  u16* wkv = (u16*)(ws + WS_WKV);
  float* csq = (float*)(ws + WS_CSQ); float* csg = (float*)(ws + WS_CSG); float* csu = (float*)(ws + WS_CSU);
  float2* st0 = (float2*)(ws + WS_ST0); float2* stA = (float2*)(ws + WS_STA); float2* stB = (float2*)(ws + WS_STB);

  constexpr int LDS_QKV = SMALL_NSTAGE * (64 * 128 + 8 * 2048) + 512;
  constexpr int LDS_RES = SMALL_NSTAGE * (64 * 128 + 5 * 2048) + 512;
  constexpr int LDS_GLU = 3 * (256 * 128 + 8 * 2048) + 2048;
  constexpr int LDS_ATT = 65536;
  (void)hipFuncSetAttribute((const void*)gemm_qkv, hipFuncAttributeMaxDynamicSharedMemorySize, LDS_QKV);
  (void)hipFuncSetAttribute((const void*)gemm_res, hipFuncAttributeMaxDynamicSharedMemorySize, LDS_RES);
  (void)hipFuncSetAttribute((const void*)gemm_glu, hipFuncAttributeMaxDynamicSharedMemorySize, LDS_GLU);
  (void)hipFuncSetAttribute((const void*)attn_kernel, hipFuncAttributeMaxDynamicSharedMemorySize, LDS_ATT);

  hipLaunchKernelGGL(k0_prep, dim3(512 + 64 + 10), dim3(256), 0, stream, x_in, wq, wk, wv, wg, wu, st0, csq, csg, csu, wkv);

  for (int layer = 0; layer < 2; ++layer) {
    const u16* xin = layer == 0 ? x_in : xB;
    GArgs a;
    a.X = xin; a.W0 = wq; a.W1 = wkv; a.res = nullptr; a.out = qs;
    a.st_in = layer == 0 ? st0 : stB; a.st_out = nullptr; a.cs0 = csq; a.cs1 = nullptr;
    a.kf = kf; a.vf = vf; a.K = DM; a.ns_in = layer == 0 ? 1 : 8;
    hipLaunchKernelGGL(gemm_qkv, dim3(32, 8), dim3(512), LDS_QKV, stream, a);
    hipLaunchKernelGGL(attn_kernel, dim3(256), dim3(1024), LDS_ATT, stream, (const u16*)qs, (const char*)kf, (const char*)vf, ob);
    a.X = ob; a.W0 = wo; a.W1 = nullptr; a.res = xin; a.out = xA; a.st_in = nullptr; a.st_out = stA;
    a.cs0 = nullptr; a.cs1 = nullptr; a.kf = nullptr; a.vf = nullptr; a.K = DM; a.ns_in = 0;
    hipLaunchKernelGGL(gemm_res, dim3(32, 8), dim3(512), LDS_RES, stream, a);
    a.X = xA; a.W0 = wg; a.W1 = wu; a.res = nullptr; a.out = hb; a.st_in = stA; a.st_out = nullptr;
    a.cs0 = csg; a.cs1 = csu; a.K = DM; a.ns_in = 8;
    hipLaunchKernelGGL(gemm_glu, dim3(8, 27), dim3(768), LDS_GLU, stream, a);
    a.X = hb; a.W0 = wd; a.W1 = nullptr; a.res = xA; a.out = layer == 0 ? xB : (u16*)d_out;
    a.st_in = nullptr; a.st_out = stB; a.cs0 = nullptr; a.cs1 = nullptr; a.K = DFF; a.ns_in = 0;
    hipLaunchKernelGGL(gemm_res, dim3(32, 8), dim3(512), LDS_RES, stream, a);
  }
}
```
